```python
import torch

def _softmax(x, axis=-1):
    return torch.softmax(x, dim=axis)
def _gelu(x):
    return torch.nn.functional.gelu(x, approximate="tanh")
def _silu(x):
    return torch.nn.functional.silu(x)
def _sigmoid(x):
    return torch.sigmoid(x)
def _layernorm(x, axis=-1, eps=1e-5):
    m = x.mean(dim=axis, keepdim=True)
    v = x.var(dim=axis, keepdim=True, unbiased=False)
    return (x - m) / torch.sqrt(v + eps)
def _scatter_add(tgt, idx, src, axis=0):
    return tgt.clone().index_add_(axis, idx, src)
def _segment_sum(src, seg, n):
    out = torch.zeros((n,) + src.shape[1:], dtype=src.dtype)
    return out.index_add_(0, seg, src)
def _pad(x, pw):
    flat = tuple(v for pair in reversed(pw) for v in pair)
    return torch.nn.functional.pad(x, flat)

kernel_name = 'interleave_layers__gqa_block_cached__gqa_block_cached__decode'

def setup_inputs():

    _w = (lambda a: (lambda t: t.to(torch.bfloat16) if t.is_floating_point() else t)(torch.as_tensor(a)))
    def _A_setup():
        B, Sq, D = 4, 1, 640
        Hq, Hkv, Sk, Dh = 32, 32, 4096, 128
        assert Hq % Hkv == 0, (Hq, Hkv)
        return {
            "x": _w((lambda *s: torch.randn(*s, dtype=torch.bfloat16) * 0.1)(B, Sq, D)),
            "k_cache": _w((lambda *s: torch.randn(*s, dtype=torch.bfloat16) * 0.8)(B, Hkv, Sk, Dh)),
            "v_cache": _w((lambda *s: torch.randn(*s, dtype=torch.bfloat16) * 3.2)(B, Hkv, Sk, Dh)),
            "wq": _w((lambda *s: torch.randn(*s, dtype=torch.bfloat16) * 0.8)(D, Hq * Dh) * (D ** -0.5)),
            "wo": _w((lambda *s: torch.randn(*s, dtype=torch.bfloat16) * 3.2)(Hq * Dh, D) * ((Hq * Dh) ** -0.5)),
        }
    _A_in = _A_setup()

    def _B_setup():
        B, Sq, D = 4, 1, 640
        Hq, Hkv, Sk, Dh = 32, 32, 4096, 128
        assert Hq % Hkv == 0, (Hq, Hkv)
        return {
            "x": _w((lambda *s: torch.randn(*s, dtype=torch.bfloat16) * 0.1)(B, Sq, D)),
            "k_cache": _w((lambda *s: torch.randn(*s, dtype=torch.bfloat16) * 1.6)(B, Hkv, Sk, Dh)),
            "v_cache": _w((lambda *s: torch.randn(*s, dtype=torch.bfloat16) * 1.6)(B, Hkv, Sk, Dh)),
            "wq": _w((lambda *s: torch.randn(*s, dtype=torch.bfloat16) * 1.6)(D, Hq * Dh) * (D ** -0.5)),
            "wo": _w((lambda *s: torch.randn(*s, dtype=torch.bfloat16) * 1.6)(Hq * Dh, D) * ((Hq * Dh) ** -0.5)),
        }
    _B_in = _B_setup()

    return {
        "A_x": _A_in["x"],
        "A_k_cache": _A_in["k_cache"],
        "A_v_cache": _A_in["v_cache"],
        "A_wq": _A_in["wq"],
        "A_wo": _A_in["wo"],
        "B_x": _B_in["x"],
        "B_k_cache": _B_in["k_cache"],
        "B_v_cache": _B_in["v_cache"],
        "B_wq": _B_in["wq"],
        "B_wo": _B_in["wo"],
    }

def reference(A_x, A_k_cache, A_v_cache, A_wq, A_wo, B_x, B_k_cache, B_v_cache, B_wq, B_wo):
    def _A_ref(x, k_cache, v_cache, wq, wo):
        B, Sq, D = x.shape
        Hq, Dh = 32, 128
        Hkv = k_cache.shape[1]
        rep = Hq // Hkv
        q = (lambda x, *d: x.permute(*d))((x @ wq).reshape(B, Sq, Hq, Dh), 0, 2, 1, 3)
        k = (lambda x, n, axis: torch.repeat_interleave(x, n, dim=axis))(k_cache, rep, axis=1)
        v = (lambda x, n, axis: torch.repeat_interleave(x, n, dim=axis))(v_cache, rep, axis=1)
        scores = torch.einsum("bhqd,bhkd->bhqk", q, k) * (Dh ** -0.5)
        p = _softmax(scores, axis=-1)
        o = torch.einsum("bhqk,bhkd->bhqd", p, v)
        return (lambda x, *d: x.permute(*d))(o, 0, 2, 1, 3).reshape(B, Sq, Hq * Dh) @ wo

    def _B_ref(x, k_cache, v_cache, wq, wo):
        B, Sq, D = x.shape
        Hq, Dh = 32, 128
        Hkv = k_cache.shape[1]
        rep = Hq // Hkv
        q = (lambda x, *d: x.permute(*d))((x @ wq).reshape(B, Sq, Hq, Dh), 0, 2, 1, 3)
        k = (lambda x, n, axis: torch.repeat_interleave(x, n, dim=axis))(k_cache, rep, axis=1)
        v = (lambda x, n, axis: torch.repeat_interleave(x, n, dim=axis))(v_cache, rep, axis=1)
        scores = torch.einsum("bhqd,bhkd->bhqk", q, k) * (Dh ** -0.5)
        p = _softmax(scores, axis=-1)
        o = torch.einsum("bhqk,bhkd->bhqd", p, v)
        return (lambda x, *d: x.permute(*d))(o, 0, 2, 1, 3).reshape(B, Sq, Hq * Dh) @ wo

    x = A_x
    x = x + _A_ref(_layernorm(x, axis=-1), A_k_cache, A_v_cache, A_wq, A_wo)
    x = x + _B_ref(_layernorm(x, axis=-1), B_k_cache, B_v_cache, B_wq, B_wo)
    x = x + _A_ref(_layernorm(x, axis=-1), A_k_cache, A_v_cache, A_wq, A_wo)
    x = x + _B_ref(_layernorm(x, axis=-1), B_k_cache, B_v_cache, B_wq, B_wo)
    return x
```

```cpp
#include <hip/hip_runtime.h>
#include <stdint.h>
#include <type_traits>

typedef unsigned v4u __attribute__((ext_vector_type(4)));
typedef unsigned v2u __attribute__((ext_vector_type(2)));
typedef float f32x4 __attribute__((ext_vector_type(4)));
typedef float f32x2 __attribute__((ext_vector_type(2)));
typedef __bf16 bf16x2 __attribute__((ext_vector_type(2)));
typedef __bf16 bf16x8 __attribute__((ext_vector_type(8)));
#define LAS __attribute__((address_space(3)))
typedef __amdgpu_buffer_rsrc_t rsrc_t;

namespace {
constexpr int NB = 4, DM = 640, NH = 32, SK = 4096;
constexpr int NWG = 256, NTHR = 512, NDW = 7;
constexpr int NT = 18;
constexpr int NLK = 14;
constexpr int NPRE = 4;
constexpr int ND = 10;
constexpr int NRE = 5;
constexpr int POL_WB = 2, POL_KA = 2, POL_VA = 2, POL_KB = 2, POL_VB = 0;
constexpr int NBURST = 6;
constexpr int NS0 = 3;
constexpr int NL0 = 3;
constexpr int NB0 = 3;
constexpr int FD = 2;
constexpr int TS = NDW * 1024;
constexpr unsigned SENT = 0xFFFFFFFFu;
constexpr int AUX_NT = 2, AUX_SC1 = 16;
constexpr float EPS = 1e-5f;
constexpr float SCL2 = 0.088388347648318440f * 1.4426950408889634f;
constexpr unsigned SPIN_CAP = 1u << 16;

constexpr int WS_EPOCH = 0, WS_ERR = 256;
constexpr int WS_FLAGS = 4096;
constexpr int PREC = 384;
constexpr int WS_PA = WS_FLAGS + 256 * 128;
constexpr int ZREC = 256;
constexpr int WS_ZR = WS_PA + 16 * 256 * PREC;
constexpr int QREC = 384;
constexpr int WS_QR = WS_ZR + 16 * 256 * ZREC;
constexpr int WS_BYTES = WS_QR + 18 * 256 * QREC;

constexpr int L_FLAG = 0;
constexpr int QSLOT = 272;
constexpr int L_QB = 64;
constexpr int L_XP = L_QB + 7 * QSLOT + 16;
constexpr int L_OB = L_XP + 1024;
constexpr int L_YB = L_OB + 256;
constexpr int L_CS = L_YB + 192;
constexpr int PART_REC = 528;
constexpr int L_PART = L_CS + 1024;
constexpr int L_EXTRA = L_PART + 2 * NDW * PART_REC;
constexpr int L_WQ = L_EXTRA + 8192;
constexpr int L_TILE = L_WQ + 40960;
constexpr int L_END = L_TILE + NDW * NLK * 1024;
static_assert(L_END <= 163840, "LDS");
static_assert((L_PART % 16) == 0 && (L_QB % 16) == 0 && (L_XP % 16) == 0 && (L_EXTRA % 16) == 0 && (L_WQ % 16) == 0 && (L_YB % 16) == 0 && (L_CS % 16) == 0, "align");

struct Params {
  const void* x0; const void* kA; const void* vA; const void* wqA; const void* woA;
  const void* kB; const void* vB; const void* wqB; const void* woB;
  void* out; void* ws;
};

#define MEMBAR() asm volatile("" ::: "memory")
#define VMWAIT(n) asm volatile("s_waitcnt vmcnt(%0)" ::"n"(n) : "memory")
#define SCHED() __builtin_amdgcn_sched_barrier(0)
#define PIN(x) asm volatile("" : "+v"(x))
#define ANY(x) asm volatile("" : "=v"(x))
#define UNI(x) __builtin_amdgcn_readfirstlane((int)(x))
constexpr unsigned OOB = 0x80000000u;

__device__ __forceinline__ float asf(unsigned u) { return __builtin_bit_cast(float, u); }
__device__ __forceinline__ unsigned asu(float f) { return __builtin_bit_cast(unsigned, f); }
__device__ __forceinline__ unsigned cvtpk(float lo, float hi) {
  bf16x2 v = {(__bf16)lo, (__bf16)hi};
  return __builtin_bit_cast(unsigned, v);
}
__device__ __forceinline__ float bflo(unsigned u) { return asf(u << 16); }
__device__ __forceinline__ float bfhi(unsigned u) { return asf(u & 0xffff0000u); }
__device__ __forceinline__ float dot2(unsigned a, unsigned b, float c) {
  return __builtin_amdgcn_fdot2_f32_bf16(__builtin_bit_cast(bf16x2, a), __builtin_bit_cast(bf16x2, b), c, false);
}
template <int CTRL> __device__ __forceinline__ float dpp(float x) {
  return asf((unsigned)__builtin_amdgcn_mov_dpp((int)asu(x), CTRL, 0xf, 0xf, true));
}
template <int CTRL> __device__ __forceinline__ unsigned dppu(unsigned x) {
  return (unsigned)__builtin_amdgcn_mov_dpp((int)x, CTRL, 0xf, 0xf, true);
}
constexpr int XOR1 = 0xB1, XOR2 = 0x4E, ROR8 = 0x128, ROR4 = 0x124, ROR2 = 0x122, ROR1 = 0x121, BCAST = 0x150;
__device__ __forceinline__ float xrow_max(float x) {
  auto s = __builtin_amdgcn_permlane16_swap(asu(x), asu(x), false, false);
  x = fmaxf(asf(s[0]), asf(s[1]));
  auto t = __builtin_amdgcn_permlane32_swap(asu(x), asu(x), false, false);
  return fmaxf(asf(t[0]), asf(t[1]));
}
__device__ __forceinline__ float xrow_sum(float x) {
  auto s = __builtin_amdgcn_permlane16_swap(asu(x), asu(x), false, false);
  x = asf(s[0]) + asf(s[1]);
  auto t = __builtin_amdgcn_permlane32_swap(asu(x), asu(x), false, false);
  return asf(t[0]) + asf(t[1]);
}
__device__ __forceinline__ float row_sum16(float x) { x += dpp<ROR8>(x); x += dpp<ROR4>(x); x += dpp<ROR2>(x); x += dpp<ROR1>(x); return x; }
__device__ __forceinline__ float wave_sum(float x) { return xrow_sum(row_sum16(x)); }
__device__ __forceinline__ f32x4 mfma16(v4u a, v4u b, f32x4 c) {
  return __builtin_amdgcn_mfma_f32_16x16x32_bf16(__builtin_bit_cast(bf16x8, a), __builtin_bit_cast(bf16x8, b), c, 0, 0, 0);
}
__device__ __forceinline__ rsrc_t mkrs(const void* p, unsigned bytes) { return __builtin_amdgcn_make_buffer_rsrc((void*)p, 0, (int)bytes, 0x00020000); }
template <int AUX = AUX_NT> __device__ __forceinline__ v4u ldnt(rsrc_t rs, unsigned voff, unsigned soff) { return __builtin_amdgcn_raw_buffer_load_b128(rs, (int)voff, (int)soff, AUX); }
__device__ __forceinline__ v4u ld128s(rsrc_t rs, unsigned voff, unsigned soff) { return __builtin_amdgcn_raw_buffer_load_b128(rs, (int)voff, (int)soff, AUX_SC1); }
__device__ __forceinline__ v2u ld64s(rsrc_t rs, unsigned voff, unsigned soff) { return __builtin_amdgcn_raw_buffer_load_b64(rs, (int)voff, (int)soff, AUX_SC1); }
__device__ __forceinline__ void st128s(v4u v, v4u desc, unsigned voff, unsigned soff) {
  asm volatile("buffer_store_dwordx4 %0, %1, %2, %3 offen sc1\n\ts_nop 1" ::"v"(v), "v"(voff), "s"(desc), "s"(soff) : "memory");
}
__device__ __forceinline__ v4u mkdesc(const void* p, unsigned bytes) {
  const unsigned long long a = (unsigned long long)p;
  return v4u{(unsigned)UNI((unsigned)a), (unsigned)UNI((unsigned)(a >> 32) & 0xffffu), bytes, 0x00020000u};
}
__device__ __forceinline__ void st64s(v2u v, rsrc_t rs, unsigned voff, unsigned soff) { __builtin_amdgcn_raw_buffer_store_b64(v, rs, (int)voff, (int)soff, AUX_SC1); }
__device__ __forceinline__ void st32s(unsigned v, rsrc_t rs, unsigned voff, unsigned soff) { __builtin_amdgcn_raw_buffer_store_b32(v, rs, (int)voff, (int)soff, AUX_SC1); }
__device__ __forceinline__ unsigned nosent(unsigned u) { return u == SENT ? SENT - 1u : u; }
__device__ __forceinline__ unsigned umax3(unsigned a, unsigned b, unsigned c) { return max(max(a, b), c); }
__device__ __forceinline__ unsigned umax4(unsigned m, v4u v) { return umax3(umax3(m, v.x, v.y), v.z, v.w); }

__device__ __forceinline__ int fresh_lane() { int ln; asm volatile("v_mbcnt_lo_u32_b32 %0, -1, 0\n\tv_mbcnt_hi_u32_b32 %0, -1, %0" : "=v"(ln)); return ln; }
typedef LAS unsigned char* lptr;
__device__ __forceinline__ v4u lds_ld128(lptr L, int off) { return *(LAS v4u*)(L + off); }
__device__ __forceinline__ void lds_st128(lptr L, int off, v4u v) { *(LAS v4u*)(L + off) = v; }
__device__ __forceinline__ unsigned lds_flag(lptr L, int idx) { return *(volatile LAS unsigned*)(L + L_FLAG + 4 * idx); }
__device__ __forceinline__ void lds_wait_ge(lptr L, int idx, unsigned target) {
  unsigned spins = 0;
  while (lds_flag(L, idx) < target) { __builtin_amdgcn_s_sleep(1); if (++spins > (1u << 22)) break; }
  MEMBAR();
}

struct Soft { unsigned w[5]; float m, l; };
struct Lc { unsigned m0, m1, m2, m3; int qa; int v; bool c1, c2, c3; };

template <int NL, class GetK>
__device__ __forceinline__ void qk_scores(GetK getK, int ltile, v4u (&pre)[4], int lxk, lptr L, int qoff, const Lc& k, float (&s)[5]) {
  v4u B[4];
  {
    int qa = k.qa;
    PIN(qa);
#pragma unroll
    for (int v = 0; v < 4; ++v) B[v] = lds_ld128(L, qoff + (k.v == v ? qa : 256));
  }
  v4u kt[NT + 5];
  if (NL == 0) kt[NT] = lds_ld128(L, lxk);
#pragma unroll
  for (int t = 0; t < 4; ++t) kt[t] = pre[t];
#pragma unroll
  for (int u = 0; u < 5; ++u) {
    f32x4 D = {0.f, 0.f, 0.f, 0.f};
#pragma unroll
    for (int v = 0; v < 4; ++v) {
      const int t = 4 * u + v;
      if (t < NT) D = mfma16(t < NL ? kt[t] : getK(t), B[v], D);
      else if (t == NT) D = mfma16(kt[NT], B[v], D);
      if (t + 4 < NL) { SCHED(); MEMBAR(); kt[t + 4] = lds_ld128(L, ltile + (t + 4) * 1024); MEMBAR(); SCHED(); }
      if (NL > 0 && t + 4 == NT) { SCHED(); MEMBAR(); kt[NT] = lds_ld128(L, lxk); MEMBAR(); SCHED(); }
    }
    float z = k.c1 ? D[1] : D[0]; z = k.c2 ? D[2] : z; z = k.c3 ? D[3] : z;
    z += dpp<XOR2>(z); z += dpp<XOR1>(z);
    s[u] = z * SCL2;
  }
}
__device__ __forceinline__ void softmax5(float (&s)[5], bool hasx, const Lc& k, Soft& S) {
  { const int nv = hasx ? 3 : 2; float ninf = -INFINITY; asm volatile("" : "+s"(ninf)); s[4] = (k.v < nv) ? s[4] : ninf; }
  float m = fmaxf(fmaxf(fmaxf(s[0], s[1]), fmaxf(s[2], s[3])), s[4]);
  m = fmaxf(m, dpp<ROR4>(m)); m = fmaxf(m, dpp<ROR8>(m)); m = xrow_max(m);
  float l = 0.f;
#pragma unroll
  for (int u = 0; u < 5; ++u) {
    const float pv = __builtin_amdgcn_exp2f(s[u] - m);
    S.w[u] = cvtpk(pv, pv);
    l += bfhi(S.w[u]);
  }
  l += dpp<ROR4>(l); l += dpp<ROR8>(l); l = xrow_sum(l);
  S.m = m; S.l = l;
}
template <int T> __device__ __forceinline__ f32x4 pv(f32x4 acc, const Soft& S, v4u V, const Lc& k) {
  const unsigned t = dppu<BCAST + 4 * (T & 3)>(S.w[T >> 2]);
  v4u A; A.x = t & k.m0; A.y = t & k.m1; A.z = t & k.m2; A.w = t & k.m3;
  return mfma16(A, V, acc);
}
__device__ __forceinline__ void put_partial(lptr L, int par, int w, int, f32x4 acc, const Soft& S) {
  const int lane = fresh_lane();
  lptr rec = L + L_PART + (par * NDW + w) * PART_REC;
  if (lane < 32) *(LAS f32x4*)(rec + 16 + (lane & 15) * 32 + (lane >> 4) * 16) = acc;
  if (lane == 0) *(LAS f32x2*)(rec) = f32x2{S.m, S.l};
  asm volatile("s_waitcnt lgkmcnt(0)" ::: "memory");
  if (lane == 0) __hip_atomic_fetch_add((LAS unsigned*)(L + L_FLAG + 4 * par), 1u, __ATOMIC_RELAXED, __HIP_MEMORY_SCOPE_WORKGROUP);
  MEMBAR();
}

template <int I, int N, class F> __device__ __forceinline__ void sfor(F f) {
  if constexpr (I < N) { f(std::integral_constant<int, I>{}); sfor<I + 1, N>(f); }
}

__device__ __forceinline__ void data_wave(const Params& p, lptr L, int w, int lane, int h, int j) {
  const rsrc_t rKA = mkrs(p.kA, 1u << 27), rVA = mkrs(p.vA, 1u << 27), rKB = mkrs(p.kB, 1u << 27), rVB = mkrs(p.vB, 1u << 27);
  const unsigned voff = lane * 16;
  const unsigned voffK = (unsigned)(((lane & 15) >> 2) * 256 + ((lane & 3) + 4 * (lane >> 4)) * 16);
  const bool hasx = w < 2;
  int ltile = L_TILE + w * NLK * 1024 + lane * 16;
  int lx = L_EXTRA + (w & 1) * 4096 + lane * 16;
  PIN(ltile); PIN(lx);
  auto kxaddr = [&](int off) { int a = lx + off; PIN(a); return hasx ? a : L_QB + 256; };
  Lc pc;
  { const int m = lane & 15; const unsigned hm = (m & 1) ? 0xffff0000u : 0x0000ffffu; const int kk = m < 8 ? (m >> 1) : -1;
    pc.m0 = kk == 0 ? hm : 0u; pc.m1 = kk == 1 ? hm : 0u; pc.m2 = kk == 2 ? hm : 0u; pc.m3 = kk == 3 ? hm : 0u;
    pc.qa = ((m & 3) + 4 * (lane >> 4)) * 16; pc.v = m >> 2; pc.c1 = (m & 3) == 1; pc.c2 = (m & 3) == 2; pc.c3 = (m & 3) == 3; }
  constexpr int NBK = NT - NLK;
  constexpr int NAV = NT - ND;
  constexpr int NLATE = NAV - NPRE;
  constexpr int LAG = NS0 > FD ? NS0 : FD;
  constexpr int LAGC = LAG < NL0 ? LAG : NL0;
  v4u RAK[NT], RAV[NAV], RBK[NBK], RBV[NT];
#pragma unroll
  for (int t = 0; t < NBK; ++t) RBK[t] = v4u{0, 0, 0, 0};
#pragma unroll
  for (int t = 0; t < NT; ++t) RBV[t] = v4u{0, 0, 0, 0};
  auto unitB = [&](int qoff, int par, v4u (&pre)[4]) {
    Soft S; f32x4 acc = {0.f, 0.f, 0.f, 0.f}; float s[5];
    qk_scores<NLK>([&](int t) { return RBK[t < NLK ? 0 : t - NLK]; }, ltile, pre, kxaddr(2048), L, qoff, pc, s);
    softmax5(s, hasx, pc, S);
    sfor<0, NT>([&](auto I) { acc = pv<I>(acc, S, RBV[I], pc); });
    if (hasx) { const v4u vx = lds_ld128(L, lx + 3072); acc = pv<NT>(acc, S, vx, pc); }
    put_partial(L, par, w, lane, acc, S);
  };

#pragma unroll 1
  for (int b = 0; b < NB; ++b) {
    const unsigned sb = (unsigned)(((b * NH + h) * SK + 512 * j + 4 * w) * 256);
    const unsigned sx = (unsigned)(((b * NH + h) * SK + 512 * j + 504 + 4 * (w & 1)) * 256);
    {
      v4u kx; ANY(kx);
      if (hasx) kx = ldnt<POL_KA>(rKA, voffK, sx);
#pragma unroll
      for (int t = 0; t < NT; ++t) { RAK[t] = ldnt<POL_KA>(rKA, voffK, sb + t * TS); VMWAIT(FD); if (t == FD && hasx) lds_st128(L, lx, kx); SCHED(); }
    }
#pragma unroll
    for (int t = 0; t < NPRE; ++t) { RAV[t] = ldnt<POL_VA>(rVA, voff, sb + t * TS); VMWAIT(FD); SCHED(); }
    Soft S1;
    f32x4 acc1 = {0.f, 0.f, 0.f, 0.f};
    {
      v4u st[ND];
      v4u vx; ANY(vx);
      if (hasx) vx = ldnt<POL_VA>(rVA, voff, sx);
#pragma unroll
      for (int t = 0; t < NS0; ++t) { st[t] = ldnt<POL_VA>(rVA, voff, sb + (NPRE + t) * TS); if (t + 1 < NS0) VMWAIT(FD); SCHED(); }
      lds_wait_ge(L, 5, (unsigned)(b + 1));
      { v4u nopre[4]; float s[5];
        qk_scores<0>([&](int t) { return RAK[t]; }, ltile, nopre, kxaddr(0), L, L_QB + b * QSLOT, pc, s);
        softmax5(s, hasx, pc, S1); }
      SCHED();
      sfor<NS0, ND + LAG>([&](auto I) {
        constexpr int t = I;
        if constexpr (t < ND) { st[t] = ldnt<POL_VA>(rVA, voff, sb + (NPRE + t) * TS); VMWAIT(FD); }
        if constexpr (t == NS0) {
          sfor<0, NPRE>([&](auto J) { acc1 = pv<J>(acc1, S1, RAV[J], pc); });
          if (hasx) { lds_st128(L, lx + 1024, vx); acc1 = pv<NT>(acc1, S1, vx, pc); }
        }
        if constexpr (t >= LAG) acc1 = pv<NPRE + t - LAG>(acc1, S1, st[t - LAG], pc);
        if constexpr (t >= ND && t - ND < NL0) RAV[NPRE + t - ND] = ldnt<POL_VA>(rVA, voff, sb + (NPRE + ND + t - ND) * TS);
        SCHED();
      });
    }
#pragma unroll
    for (int t = LAGC; t < NLATE; ++t) { RAV[NPRE + t] = ldnt<POL_VA>(rVA, voff, sb + (NPRE + ND + t) * TS); if (t + 1 < NLATE) VMWAIT(FD); SCHED(); }
    sfor<0, NLATE>([&](auto I) { acc1 = pv<NPRE + ND + I>(acc1, S1, RAV[NPRE + I], pc); });
    put_partial(L, b & 1, w, lane, acc1, S1);
    SCHED();
    {
      v4u st[NT];
#pragma unroll
      for (int t = 0; t < NB0; ++t) { st[t] = ldnt<POL_KB>(rKB, voffK, sb + t * TS); SCHED(); }
      if (b > 0) {
        v4u pre[4];
#pragma unroll
        for (int t = 0; t < 4; ++t) pre[t] = lds_ld128(L, ltile + t * 1024);
        lds_wait_ge(L, 4, (unsigned)b);
        unitB(L_QB + 6 * QSLOT, (b - 1) & 1, pre);
      }
      SCHED();
      lds_wait_ge(L, 6, 1u);
      v4u kx, vx; ANY(kx); ANY(vx);
      if (hasx) kx = ldnt<POL_KB>(rKB, voffK, sx);
      constexpr int LB = NB0 > FD ? NB0 : FD;
#pragma unroll
      for (int t = NB0; t < NT + LB; ++t) {
        if (t < NT) { if (t < NLK) st[t] = ldnt<POL_KB>(rKB, voffK, sb + t * TS); else RBK[t - NLK] = ldnt<POL_KB>(rKB, voffK, sb + t * TS); VMWAIT(FD); }
        else if (t - NT < NT) { RBV[t - NT] = ldnt<POL_VB>(rVB, voff, sb + (t - NT) * TS); VMWAIT(FD); }
        if (t == NB0 + FD && hasx) lds_st128(L, lx + 2048, kx);
        if (t >= LB && t - LB < NLK) lds_st128(L, ltile + (t - LB) * 1024, st[t - LB]);
        SCHED();
      }
      if (hasx) vx = ldnt<POL_VB>(rVB, voff, sx);
#pragma unroll
      for (int t = LB; t < NT; ++t) { RBV[t] = ldnt<POL_VB>(rVB, voff, sb + t * TS); if (t + 1 < NT - NBURST) VMWAIT(FD); if (t == LB + FD && hasx) lds_st128(L, lx + 3072, vx); SCHED(); }
      v4u pre[4];
#pragma unroll
      for (int t = 0; t < 4; ++t) pre[t] = lds_ld128(L, ltile + t * 1024);
      lds_wait_ge(L, 2, (unsigned)(b + 1));
      unitB(L_QB + 4 * QSLOT, b & 1, pre);
    }
    SCHED();
    {
      v4u st[ND];
#pragma unroll
      for (int t = 0; t < NRE; ++t) st[t] = ldnt<POL_VA>(rVA, voff, sb + (NPRE + t) * TS);
      SCHED();
      lds_wait_ge(L, 3, (unsigned)(b + 1));
      Soft S; f32x4 acc = {0.f, 0.f, 0.f, 0.f}; float s[5]; v4u nopre[4];
      qk_scores<0>([&](int t) { return RAK[t]; }, ltile, nopre, kxaddr(0), L, L_QB + 5 * QSLOT, pc, s);
      SCHED();
#pragma unroll
      for (int t = NRE; t < ND; ++t) st[t] = ldnt<POL_VA>(rVA, voff, sb + (NPRE + t) * TS);
      SCHED();
      softmax5(s, hasx, pc, S);
      sfor<0, NPRE>([&](auto I) { acc = pv<I>(acc, S, RAV[I], pc); });
      sfor<0, NAV - NPRE>([&](auto I) { acc = pv<NPRE + ND + I>(acc, S, RAV[NPRE + I], pc); });
      if (hasx) { const v4u vx = lds_ld128(L, lx + 1024); acc = pv<NT>(acc, S, vx, pc); }
      sfor<0, ND>([&](auto I) { acc = pv<NPRE + I>(acc, S, st[I], pc); });
      PIN(acc);
      put_partial(L, b & 1, w, lane, acc, S);
    }
    SCHED();
  }
  {
    v4u pre[4];
#pragma unroll
    for (int t = 0; t < 4; ++t) pre[t] = lds_ld128(L, ltile + t * 1024);
    lds_wait_ge(L, 4, (unsigned)NB);
    unitB(L_QB + 6 * QSLOT, (NB - 1) & 1, pre);
  }
}

struct Comm {
  rsrc_t rs; v4u rd; lptr L; int h, j; unsigned cnt0, cnt1; unsigned dead; int lazy; unsigned m0, m1, m2, m3;
};
constexpr int LAZY_SLEEP = 12, FAST_SLEEP = 1;
constexpr int DLY_P = 30, DLY_Z = 40, DLY_Q = 25, DLY_P3 = 12;
constexpr int DLY_P2 = 16, DLY_Z2 = 16, DLY_Q2 = 12;
__device__ __forceinline__ bool spin_fail(Comm& C, unsigned& spins, unsigned code) {
  if (C.dead) return true;
  if (C.lazy == LAZY_SLEEP) __builtin_amdgcn_s_sleep(LAZY_SLEEP); else __builtin_amdgcn_s_sleep(FAST_SLEEP);
  if (++spins > SPIN_CAP) { C.dead = 1; st32s(code, C.rs, fresh_lane() == 0 ? 0u : OOB, WS_ERR); return true; }
  return false;
}
__device__ __forceinline__ void merge_publish(Comm& C, int par, int s) {
  const int lane = fresh_lane();
  unsigned& cnt = par ? C.cnt1 : C.cnt0;
  cnt += NDW;
  lds_wait_ge(C.L, par, cnt);
  lptr rec = C.L + L_PART + par * NDW * PART_REC;
  f32x2 ml[NDW], a[NDW];
#pragma unroll
  for (int w = 0; w < NDW; ++w) { ml[w] = *(LAS f32x2*)(rec + w * PART_REC); a[w] = *(LAS f32x2*)(rec + w * PART_REC + 16 + lane * 8); }
  float M = ml[0].x;
#pragma unroll
  for (int w = 1; w < NDW; ++w) M = fmaxf(M, ml[w].x);
  float Ls = 0.f, o0 = 0.f, o1 = 0.f;
#pragma unroll
  for (int w = 0; w < NDW; ++w) { const float e = __builtin_amdgcn_exp2f(ml[w].x - M); Ls = fmaf(e, ml[w].y, Ls); o0 = fmaf(e, a[w].x, o0); o1 = fmaf(e, a[w].y, o1); }
  const unsigned d = nosent(cvtpk(o0, o1));
  const v4u dv = {dppu<0x00>(d), dppu<0x55>(d), dppu<0xAA>(d), dppu<0xFF>(d)};
  const unsigned pb = (unsigned)UNI(WS_PA + ((s * 32 + C.h) * 8 + C.j) * PREC);
  st128s(dv, C.rd, (lane & 3) == 0 ? (unsigned)(lane * 4) : OOB, pb);
  st64s(v2u{nosent(asu(M)), nosent(asu(Ls))}, C.rs, lane == 0 ? 256u : OOB, pb);
}
__device__ __forceinline__ v4u diag_ones(const Comm& C) {
  unsigned ones = 0x3F803F80u;
  asm volatile("" : "+s"(ones));
  return v4u{ones & C.m0, ones & C.m1, ones & C.m2, ones & C.m3};
}
__device__ __forceinline__ void outproj(Comm& C, int s, const v4u (&WO)[4][5]) {
  const int lane = fresh_lane(), r = lane >> 4, c = lane & 15;
  const unsigned rb = (unsigned)UNI(WS_PA + (s * 32 + C.h) * 8 * PREC);
  v2u ml; v4u a0, a1;
  unsigned spins = 0;
  for (;;) {
    MEMBAR();
    ml = ld64s(C.rs, (4 * (c & 1) + r) * PREC + 256, rb);
    a0 = ld128s(C.rs, r * PREC + c * 16, rb);
    a1 = ld128s(C.rs, r * PREC + c * 16, rb + 4 * PREC);
    const unsigned mx = umax4(umax4(max(ml.x, ml.y), a0), a1);
    if (!__any(mx == SENT)) break;
    if (spin_fail(C, spins, 0x100u + s)) break;
  }
  const float m = asf(ml.x), l = asf(ml.y);
  const float M = xrow_max(fmaxf(m, dpp<XOR1>(m)));
  const float e = __builtin_amdgcn_exp2f(m - M);
  const unsigned eb = cvtpk(e, e);
  float Ls = bfhi(eb) * l; Ls += dpp<XOR1>(Ls); Ls = xrow_sum(Ls);
  v4u A; const unsigned w0 = dppu<BCAST + 0>(eb), w1 = dppu<BCAST + 1>(eb);
  A.x = w0 & C.m0; A.y = w0 & C.m1; A.z = w0 & C.m2; A.w = w0 & C.m3;
  f32x4 D = mfma16(A, a0, f32x4{0.f, 0.f, 0.f, 0.f});
  A.x = w1 & C.m0; A.y = w1 & C.m1; A.z = w1 & C.m2; A.w = w1 & C.m3;
  D = mfma16(A, a1, D);
  const float inv = __builtin_amdgcn_rcpf(Ls);
  const v2u ob = {cvtpk(D[0] * inv, D[1] * inv), cvtpk(D[2] * inv, D[3] * inv)};
  if (lane < 32) *(LAS v2u*)(C.L + L_OB + c * 16 + r * 8) = ob;
  MEMBAR();
  f32x4 Z[5];
#pragma unroll
  for (int k = 0; k < 4; ++k) {
    const v4u o = lds_ld128(C.L, L_OB + k * 64 + r * 16);
#pragma unroll
    for (int n = 0; n < 5; ++n) Z[n] = mfma16(o, WO[k][n], k == 0 ? f32x4{0.f, 0.f, 0.f, 0.f} : Z[n]);
  }
  const v4u v = {nosent(cvtpk(Z[0][0], Z[1][0])), nosent(cvtpk(Z[2][0], Z[3][0])), nosent(cvtpk(Z[4][0], 0.f)), 0u};
  st128s(v, C.rd, lane < 16 ? (unsigned)(lane * 16) : OOB, (unsigned)UNI(WS_ZR + ((s * 8 + C.j) * 32 + C.h) * ZREC));
}
__device__ __forceinline__ void residual(Comm& C, int s, v2u& x) {
  const int lane = fresh_lane();
  v4u a[8];
  unsigned spins = 0;
  const unsigned zb = (unsigned)UNI(WS_ZR + (s * 8 + C.j) * 32 * ZREC);
  for (;;) {
    MEMBAR();
#pragma unroll
    for (int k = 0; k < 8; ++k) a[k] = ld128s(C.rs, lane * 16, zb + k * 1024);
    unsigned mx = 0;
#pragma unroll
    for (int k = 0; k < 8; ++k) mx = umax4(mx, a[k]);
    if (!__any(mx == SENT)) break;
    if (spin_fail(C, spins, 0x200u + s)) break;
  }
  const v4u A = diag_ones(C);
  f32x4 D0 = mfma16(A, a[0], f32x4{0.f, 0.f, 0.f, 0.f}), D1 = mfma16(A, a[1], f32x4{0.f, 0.f, 0.f, 0.f});
#pragma unroll
  for (int k = 2; k < 8; k += 2) { D0 = mfma16(A, a[k], D0); D1 = mfma16(A, a[k + 1], D1); }
  const unsigned at0 = cvtpk(D0[0] + D1[0], D0[1] + D1[1]), at1 = cvtpk(D0[2] + D1[2], D0[3] + D1[3]);
  x.x = cvtpk(bflo(x.x) + bflo(at0), bfhi(x.x) + bfhi(at0));
  x.y = cvtpk(bflo(x.y) + bflo(at1), bfhi(x.y) + bfhi(at1));
}
template <bool ONES>
__device__ __forceinline__ void qpart(Comm& C, v2u x, int wqoff, int sq) {
  const int lane = fresh_lane(), r = lane >> 4, c = lane & 15;
  unsigned ones = 0x3F803F80u;
  asm volatile("" : "+s"(ones));
  float sy = 0.f, sy2 = 0.f;
  v4u y[3];
  v4u wv[24];
  auto rd = [&](int i) { const int hf = i / 12, s = (i % 12) / 4, cb = 4 * hf + (i & 3);
    wv[i] = s < 2 ? lds_ld128(C.L, wqoff + (s * 8 + cb) * 1024 + lane * 16) : lds_ld128(C.L, wqoff + 16384 + cb * 512 + (lane & 31) * 16); };
  MEMBAR();
#pragma unroll
  for (int i = 0; i < 8; ++i) rd(i);
  MEMBAR();
  if (!ONES) {
    if (r == 0) { *(LAS unsigned*)(C.L + L_YB + c * 4) = x.x; *(LAS unsigned*)(C.L + L_YB + 64 + c * 4) = x.y; }
    if (r == 1) *(LAS unsigned short*)(C.L + L_YB + 128 + c * 2) = (unsigned short)x.x;
    MEMBAR();
#pragma unroll
    for (int k = 0; k < 3; ++k) y[k] = lds_ld128(C.L, L_YB + k * 64 + r * 16);
    sy = wave_sum(dot2(x.x, ones, dot2(x.y, ones, 0.f)));
    sy2 = wave_sum(dot2(x.x, x.x, dot2(x.y, x.y, 0.f)));
  } else {
    const unsigned o2 = r < 2 ? ones : 0u;
    y[0] = y[1] = v4u{ones, ones, ones, ones}; y[2] = v4u{o2, o2, o2, o2};
  }
  v4u v;
#pragma unroll
  for (int hf = 0; hf < 2; ++hf) {
    f32x4 Q[4];
#pragma unroll
    for (int s = 0; s < 3; ++s) {
      const int i0 = 12 * hf + 4 * s;
      if (i0 + 8 < 24) { MEMBAR();
#pragma unroll
        for (int t = 0; t < 4; ++t) rd(i0 + 8 + t);
        MEMBAR(); }
#pragma unroll
      for (int t = 0; t < 4; ++t) Q[t] = mfma16(y[s], wv[i0 + t], s == 0 ? f32x4{0.f, 0.f, 0.f, 0.f} : Q[t]);
      SCHED();
    }
    v[2 * hf] = nosent(cvtpk(Q[0][0], Q[1][0])); v[2 * hf + 1] = nosent(cvtpk(Q[2][0], Q[3][0]));
  }
  const unsigned rb = (unsigned)UNI(WS_QR + ((sq * 32 + C.h) * 8 + C.j) * QREC);
  st128s(v, C.rd, lane < 16 ? (unsigned)(c * 16) : OOB, rb);
  st64s(v2u{nosent(asu(sy)), nosent(asu(sy2))}, C.rs, lane == 0 ? 256u : OOB, rb);
}
template <int MODE>
__device__ __forceinline__ void qgather(Comm& C, int sq, int cs, int slot, int fidx, unsigned val) {
  const int lane = fresh_lane(), r = lane >> 4, c = lane & 15;
  const unsigned rb = (unsigned)UNI(WS_QR + (sq * 32 + C.h) * 8 * QREC);
  v4u a0, a1; v2u st;
  unsigned spins = 0;
  for (;;) {
    MEMBAR();
    a0 = ld128s(C.rs, r * QREC + c * 16, rb);
    a1 = ld128s(C.rs, r * QREC + c * 16, rb + 4 * QREC);
    st = ld64s(C.rs, (lane & 7) * QREC + 256, rb);
    const unsigned mx = umax4(umax4(max(st.x, st.y), a0), a1);
    if (!__any(mx == SENT)) break;
    if (spin_fail(C, spins, 0x300u + sq)) break;
  }
  const v4u A = diag_ones(C);
  const f32x4 q = mfma16(A, a1, mfma16(A, a0, f32x4{0.f, 0.f, 0.f, 0.f}));
  if (MODE == 1) {
    if (lane < 32) *(LAS f32x4*)(C.L + slot + lane * 16) = q;
    asm volatile("s_waitcnt lgkmcnt(0)" ::: "memory");
  } else {
    const f32x4 cv = *(LAS f32x4*)(C.L + cs + (lane & 31) * 16);
    float sy = asf(st.x), sy2 = asf(st.y);
    sy += dpp<ROR4>(sy); sy += dpp<XOR2>(sy); sy += dpp<XOR1>(sy);
    sy2 += dpp<ROR4>(sy2); sy2 += dpp<XOR2>(sy2); sy2 += dpp<XOR1>(sy2);
    const float mean = sy * (1.f / DM);
    const float var = fmaxf(sy2 * (1.f / DM) - mean * mean, 0.f);
    const float rstd = __builtin_amdgcn_rsqf(var + EPS);
    const v2u qb = {cvtpk((q[0] - mean * cv[0]) * rstd, (q[1] - mean * cv[1]) * rstd), cvtpk((q[2] - mean * cv[2]) * rstd, (q[3] - mean * cv[3]) * rstd)};
    if (lane < 32) *(LAS v2u*)(C.L + L_QB + slot * QSLOT + c * 16 + r * 8) = qb;
    asm volatile("s_waitcnt lgkmcnt(0)" ::: "memory");
    if (lane == 0) *(volatile LAS unsigned*)(C.L + L_FLAG + 4 * fidx) = val;
    MEMBAR();
  }
}
__device__ __forceinline__ void wo_issue(lptr L, int off, int lane, unsigned (&P0)[4][8], unsigned (&P1)[4][8], unsigned (&P2)[4][8]) {
  const int g = lane >> 4, n = lane & 15;
  const int base = off + 8 * g * 160;
#pragma unroll
  for (int s = 0; s < 4; ++s)
#pragma unroll
    for (int e = 0; e < 8; ++e) {
      const int row = (32 * s + e) * 160;
      P0[s][e] = *(LAS unsigned*)(L + base + row + 4 * n);
      P1[s][e] = *(LAS unsigned*)(L + base + row + 64 + 4 * n);
      P2[s][e] = *(LAS unsigned short*)(L + base + row + 128 + 2 * n);
    }
}
__device__ __forceinline__ void wo_convert(const unsigned (&P0)[4][8], const unsigned (&P1)[4][8], const unsigned (&P2)[4][8], v4u (&W)[4][5]) {
#pragma unroll
  for (int s = 0; s < 4; ++s) {
#pragma unroll
    for (int ep = 0; ep < 4; ++ep) {
      W[s][0][ep] = __builtin_amdgcn_perm(P0[s][2 * ep + 1], P0[s][2 * ep], 0x05040100u);
      W[s][1][ep] = __builtin_amdgcn_perm(P0[s][2 * ep + 1], P0[s][2 * ep], 0x07060302u);
      W[s][2][ep] = __builtin_amdgcn_perm(P1[s][2 * ep + 1], P1[s][2 * ep], 0x05040100u);
      W[s][3][ep] = __builtin_amdgcn_perm(P1[s][2 * ep + 1], P1[s][2 * ep], 0x07060302u);
      W[s][4][ep] = __builtin_amdgcn_perm(P2[s][2 * ep + 1], P2[s][2 * ep], 0x05040100u);
    }
#pragma unroll
    for (int n5 = 0; n5 < 5; ++n5) PIN(W[s][n5]);
  }
  SCHED(); MEMBAR();
}
__device__ __forceinline__ void wq_issue(lptr L, int off, int lane, v4u (&A)[3][8]) {
#pragma unroll
  for (int s = 0; s < 3; ++s)
#pragma unroll
    for (int e = 0; e < 8; ++e) A[s][e] = lds_ld128(L, off + (32 * s + 8 * (lane >> 4) + e) * 256 + (lane & 15) * 16);
}
__device__ __forceinline__ void wq_store(const v4u (&A)[3][8], lptr L, int off, int lane) {
#pragma unroll
  for (int s = 0; s < 3; ++s)
#pragma unroll
    for (int cb = 0; cb < 8; ++cb) {
      const unsigned sel = (cb & 1) ? 0x07060302u : 0x05040100u;
      v4u v;
#pragma unroll
      for (int e = 0; e < 4; ++e) v[e] = __builtin_amdgcn_perm(A[s][2 * e + 1][cb >> 1], A[s][2 * e][cb >> 1], sel);
      if (s < 2) lds_st128(L, off + (s * 8 + cb) * 1024 + lane * 16, v);
      else if (lane < 32) lds_st128(L, off + 16384 + cb * 512 + lane * 16, v);
    }
  asm volatile("s_waitcnt lgkmcnt(0)" ::: "memory");
}
__device__ __forceinline__ v2u x_load(const Params& p, int b, int j, int) {
  const int lane = fresh_lane();
  const rsrc_t rx = mkrs(p.x0, NB * DM * 2);
  const int r = lane >> 4, c = lane & 15;
  const unsigned a = __builtin_amdgcn_raw_buffer_load_b32(rx, r == 0 ? c * 4 : (int)OOB, UNI(b * 1280 + j * 160), 0);
  const unsigned bb = __builtin_amdgcn_raw_buffer_load_b32(rx, r == 0 ? 64 + c * 4 : (int)OOB, UNI(b * 1280 + j * 160), 0);
  const unsigned cc = __builtin_amdgcn_raw_buffer_load_b16(rx, r == 1 ? 128 + c * 2 : (int)OOB, UNI(b * 1280 + j * 160), 0);
  return v2u{a | cc, bb};
}
__device__ __forceinline__ void out_store(const Params& p, int b, int j, int, v2u x) {
  const int lane = fresh_lane();
  const rsrc_t ro = mkrs(p.out, NB * DM * 2);
  const int r = lane >> 4, c = lane & 15;
  __builtin_amdgcn_raw_buffer_store_b32(x.x, ro, r == 0 ? c * 4 : (int)OOB, UNI(b * 1280 + j * 160), 0);
  __builtin_amdgcn_raw_buffer_store_b32(x.y, ro, r == 0 ? 64 + c * 4 : (int)OOB, UNI(b * 1280 + j * 160), 0);
  __builtin_amdgcn_raw_buffer_store_b16((unsigned short)x.x, ro, r == 1 ? 128 + c * 2 : (int)OOB, UNI(b * 1280 + j * 160), 0);
}

template <int N> __device__ __forceinline__ void nap() { if (N > 0) __builtin_amdgcn_s_sleep(N > 127 ? 127 : N); }
__device__ __forceinline__ void comm_wave(const Params& p, lptr L, int lane, int g, int i, int h, int j) {
  __builtin_amdgcn_s_setprio(3);
  Comm C;
  C.rs = mkrs(p.ws, WS_BYTES); C.rd = mkdesc(p.ws, WS_BYTES); C.L = L; C.h = h; C.j = j; C.cnt0 = 0; C.cnt1 = 0;
  C.dead = 0; C.lazy = LAZY_SLEEP;
  { const int m = lane & 15; const unsigned hm = (m & 1) ? 0xffff0000u : 0x0000ffffu; const int kk = m < 8 ? (m >> 1) : -1;
    C.m0 = kk == 0 ? hm : 0u; C.m1 = kk == 1 ? hm : 0u; C.m2 = kk == 2 ? hm : 0u; C.m3 = kk == 3 ? hm : 0u; }
  const int cu = blockIdx.x;
  v4u WOA[4][5], WOB[4][5];
  v2u tg;
  {
    const v2u ep = ld64s(C.rs, 0, WS_EPOCH);
    {
      const v4u s4 = {SENT, SENT, SENT, SENT};
      const int me = h * 8 + j;
#pragma unroll
      for (int k = 0; k < 4; ++k) st128s(s4, C.rd, (unsigned)(((4 * k + (lane >> 4)) * 256 + me) * PREC + (lane & 15) * 16), WS_PA);
      st64s(v2u{SENT, SENT}, C.rs, lane < 16 ? (unsigned)((lane * 256 + me) * PREC + 256) : OOB, WS_PA);
#pragma unroll
      for (int k = 0; k < 4; ++k) st128s(s4, C.rd, (unsigned)((((4 * k + (lane >> 4)) * 8 + j) * 32 + h) * ZREC + (lane & 15) * 16), WS_ZR);
#pragma unroll
      for (int k = 0; k < 5; ++k) { const int stg = 4 * k + (lane >> 4); st128s(s4, C.rd, stg < 18 ? (unsigned)((stg * 256 + me) * QREC + (lane & 15) * 16) : OOB, WS_QR); }
      st64s(v2u{SENT, SENT}, C.rs, lane < 18 ? (unsigned)((lane * 256 + me) * QREC + 256) : OOB, WS_QR);
    }
    SCHED(); MEMBAR();
    const v2u x0 = x_load(p, 0, j, lane);
    SCHED(); MEMBAR();
    { v4u RQ[3][8]; wq_issue(L, L_TILE, lane, RQ); wq_store(RQ, L, L_WQ, lane); }
    VMWAIT(0);
    const unsigned long long tag = (((unsigned long long)ep.y << 32) | ep.x) + 1ull;
    tg = v2u{(unsigned)tag, (unsigned)(tag >> 32)};
    st64s(tg, C.rs, lane == 0 ? 0u : OOB, (unsigned)UNI(WS_FLAGS + cu * 128));
    qpart<false>(C, x0, L_WQ, 0);
    qpart<true>(C, x0, L_WQ, 16);
    { v4u RB[3][8]; wq_issue(L, L_TILE + 2 * 20480, lane, RB); wq_store(RB, L, L_WQ + 20480, lane); }
    qpart<true>(C, x0, L_WQ + 20480, 17);
    { unsigned A0[4][8], A1[4][8], A2[4][8]; wo_issue(L, L_TILE + 20480, lane, A0, A1, A2); wo_convert(A0, A1, A2, WOA); }
    { unsigned B0[4][8], B1[4][8], B2[4][8]; wo_issue(L, L_TILE + 3 * 20480, lane, B0, B1, B2); wo_convert(B0, B1, B2, WOB); }
    asm volatile("s_waitcnt lgkmcnt(0)" ::: "memory");
    if (lane == 0) *(volatile LAS unsigned*)(L + L_FLAG + 4 * 6) = 1u;
    for (unsigned spins = 0;;) {
      MEMBAR();
      const int hp = lane & 31;
      const int other = lane < 32 ? 64 * (h & 3) + 8 * (lane & 7) + g : 64 * (hp & 3) + 8 * j + (hp >> 2);
      const v2u f = ld64s(C.rs, other * 128, WS_FLAGS);
      if (__all(f.x == tg.x && f.y == tg.y)) break;
      if (spin_fail(C, spins, 0x400u)) break;
    }
    C.lazy = FAST_SLEEP;
    qgather<1>(C, 16, 0, L_CS, 0, 0);
    qgather<0>(C, 0, L_CS, 0, 5, 1u);
    C.lazy = LAZY_SLEEP;
    qgather<1>(C, 17, 0, L_CS + 512, 0, 0);
    if (cu == 0) {
      for (unsigned spins = 0;;) {
        MEMBAR();
        bool ok = true;
#pragma unroll
        for (int k = 0; k < 4; ++k) { const v2u f = ld64s(C.rs, lane * 128, WS_FLAGS + k * 8192); ok = ok && f.x == tg.x && f.y == tg.y; }
        if (__all(ok)) break;
        if (spin_fail(C, spins, 0x401u)) break;
      }
      st64s(tg, C.rs, lane == 0 ? 0u : OOB, WS_EPOCH);
    }
  }
  auto final_row = [&](int bb) {
    v2u xp = *(LAS v2u*)(L + L_XP + (bb & 1) * 512 + fresh_lane() * 8);
    residual(C, 12 + bb, xp);
    if (C.dead) xp = v2u{0x7FC07FC0u, 0x7FC07FC0u};
    out_store(p, bb, j, lane, xp);
  };
#pragma unroll 1
  for (int b = 0; b < NB; ++b) {
    v2u x = x_load(p, b, j, lane);
    C.lazy = LAZY_SLEEP;
    merge_publish(C, b & 1, b);
    nap<DLY_P>();
    outproj(C, b, WOA);
    if (b > 0) merge_publish(C, (b - 1) & 1, 12 + b - 1);
    nap<DLY_Z>();
    residual(C, b, x); qpart<false>(C, x, L_WQ + 20480, 4 + b);
    nap<DLY_Q>();
    qgather<0>(C, 4 + b, L_CS + 512, 4, 2, (unsigned)(b + 1));
    C.lazy = FAST_SLEEP;
    merge_publish(C, b & 1, 4 + b);
    nap<DLY_P2>();
    outproj(C, 4 + b, WOB);
    nap<DLY_Z2>();
    residual(C, 4 + b, x);
    qpart<false>(C, x, L_WQ, 8 + b);
    nap<DLY_Q2>();
    qgather<0>(C, 8 + b, L_CS, 5, 3, (unsigned)(b + 1));
    if (b + 1 < NB) { const v2u xn = x_load(p, b + 1, j, lane); qpart<false>(C, xn, L_WQ, b + 1); }
    if (b >= 2 && h == b - 2) final_row(b - 2);
    C.lazy = (b + 1 < NB) ? LAZY_SLEEP : FAST_SLEEP;
    merge_publish(C, b & 1, 8 + b);
    if (b + 1 < NB) { qgather<0>(C, b + 1, L_CS, b + 1, 5, (unsigned)(b + 2)); nap<DLY_P3>(); } else nap<DLY_P2>();
    outproj(C, 8 + b, WOA);
    if (b + 1 < NB) nap<DLY_Z>(); else nap<DLY_Z2>();
    residual(C, 8 + b, x); qpart<false>(C, x, L_WQ + 20480, 12 + b);
    if (b + 1 < NB) nap<DLY_Q>(); else nap<DLY_Q2>();
    qgather<0>(C, 12 + b, L_CS + 512, 6, 4, (unsigned)(b + 1));
    *(LAS v2u*)(L + L_XP + (b & 1) * 512 + fresh_lane() * 8) = x;
    if (b > 0) outproj(C, 12 + b - 1, WOB);
  }
  C.lazy = FAST_SLEEP;
  merge_publish(C, (NB - 1) & 1, 12 + NB - 1);
  nap<DLY_P2>();
  outproj(C, 12 + NB - 1, WOB);
  nap<DLY_Z2>();
  if (h == NB - 2) final_row(NB - 2);
  if (h == NB - 1) final_row(NB - 1);
}

__global__ void __launch_bounds__(NTHR, 2) fused_decode(Params p) {
  __shared__ __attribute__((aligned(16))) unsigned char smem[L_END];
  lptr L = (lptr)smem;
  const int tid = threadIdx.x, w = __builtin_amdgcn_readfirstlane(tid >> 6);
  const int cu = blockIdx.x, g = cu & 7, i = cu >> 3, h = 4 * g + (i >> 3), j = i & 7;
  {
    const int lane = tid & 63;
    const rsrc_t rqa = mkrs(p.wqA, 640 * 4096 * 2), rqb = mkrs(p.wqB, 640 * 4096 * 2), roa = mkrs(p.woA, 4096 * 640 * 2), rob = mkrs(p.woB, 4096 * 640 * 2);
    v4u t[4][3];
#pragma unroll
    for (int m = 0; m < 4; ++m)
#pragma unroll
      for (int k = 0; k < 3; ++k) {
        const int id = 64 * (w + 8 * k) + lane;
        const int row = (m & 1) ? (id * 6554) >> 16 : id >> 4, c = (m & 1) ? id - 10 * row : id & 15;
        const int off = (m & 1) ? (128 * h + row) * 1280 + 160 * j + 16 * c : (80 * j + row) * 8192 + 256 * h + 16 * c;
        t[m][k] = m < 2 ? __builtin_amdgcn_raw_buffer_load_b128(m == 0 ? rqa : roa, id < 1280 ? off : (int)OOB, 0, AUX_NT) : __builtin_amdgcn_raw_buffer_load_b128(m == 2 ? rqb : rob, id < 1280 ? off : (int)OOB, 0, POL_WB);
      }
    if (tid < 16) *(LAS unsigned*)(L + L_FLAG + 4 * tid) = 0u;
    if (tid >= 64 && tid < 64 + 28) { const int e = tid - 64; *(LAS unsigned*)(L + L_QB + (e >> 2) * QSLOT + 256 + (e & 3) * 4) = 0u; }
    if (tid >= 128 && tid < 136) *(LAS unsigned*)(L + L_YB + 160 + (tid - 128) * 4) = 0u;
#pragma unroll
    for (int m = 0; m < 4; ++m)
#pragma unroll
      for (int k = 0; k < 3; ++k) { const int id = 64 * (w + 8 * k) + lane; if (id < 1280) lds_st128(L, L_TILE + m * 20480 + id * 16, t[m][k]); }
  }
  __syncthreads();
  if (w == NDW) comm_wave(p, L, fresh_lane(), g, i, h, j);
  else data_wave(p, L, w, fresh_lane(), h, j);
}
}

extern "C" void kernel_launch(void* const* d_in, const int* in_sizes, int n_in, void* d_out, int out_size, void* d_ws, size_t ws_size, hipStream_t stream) {
  Params p;
  p.x0 = d_in[0]; p.kA = d_in[1]; p.vA = d_in[2]; p.wqA = d_in[3]; p.woA = d_in[4];
  p.kB = d_in[6]; p.vB = d_in[7]; p.wqB = d_in[8]; p.woB = d_in[9];
  p.out = d_out; p.ws = d_ws;
  hipLaunchKernelGGL(fused_decode, dim3(NWG), dim3(NTHR), 0, stream, p);
}
```
